```python
import torch

def _softmax(x, axis=-1):
    return torch.softmax(x, dim=axis)
def _gelu(x):
    return torch.nn.functional.gelu(x, approximate="tanh")
def _silu(x):
    return torch.nn.functional.silu(x)
def _sigmoid(x):
    return torch.sigmoid(x)
def _layernorm(x, axis=-1, eps=1e-5):
    m = x.mean(dim=axis, keepdim=True)
    v = x.var(dim=axis, keepdim=True, unbiased=False)
    return (x - m) / torch.sqrt(v + eps)
def _scatter_add(tgt, idx, src, axis=0):
    return tgt.clone().index_add_(axis, idx, src)
def _segment_sum(src, seg, n):
    out = torch.zeros((n,) + src.shape[1:], dtype=src.dtype)
    return out.index_add_(0, seg, src)
def _pad(x, pw):
    flat = tuple(v for pair in reversed(pw) for v in pair)
    return torch.nn.functional.pad(x, flat)

kernel_name = 'gather_scatter__isomorphism_gin_2__graph'

def setup_inputs():

    _w = (lambda a: (lambda t: t.to(torch.bfloat16) if t.is_floating_point() else t)(torch.as_tensor(a)))
    N, deg, D = 10000, 16, 256
    E = N * deg
    H = 2 * D
    src = (lambda lo, hi, *s: torch.randint(lo, hi, s))(0, N, E).to(torch.int64)
    dst = (lambda lo, hi, *s: torch.randint(lo, hi, s))(0, N, E).to(torch.int64)
    return {
        "x": _w((lambda *s: torch.randn(*s, dtype=torch.bfloat16) * 0.05)(N, D)),
        "src": _w(src),
        "dst": _w(dst),
        "eps": _w((lambda *s: torch.randn(*s, dtype=torch.bfloat16) * 0.05)(1) * 0.1),
        "w1": _w((lambda *s: torch.randn(*s, dtype=torch.bfloat16) * 0.05)(D, H) * D ** -0.5),
        "b1": _w((lambda *s: torch.randn(*s, dtype=torch.bfloat16) * 0.4)(H) * 0.01),
        "w2": _w((lambda *s: torch.randn(*s, dtype=torch.bfloat16) * 0.05)(H, D) * H ** -0.5),
        "b2": _w((lambda *s: torch.randn(*s, dtype=torch.bfloat16) * 0.05)(D) * 0.01),
    }

def reference(x, src, dst, eps, w1, b1, w2, b2):
    N = x.shape[0]
    agg = _segment_sum(x[src], dst, N)
    h = (1.0 + eps) * x + agg
    h = torch.einsum('nd,dh->nh', h, w1) + b1
    h = torch.where(h > 0, h, 0.0 * h)
    h = torch.einsum('nh,hd->nd', h, w2) + b2
    return h
```

```cpp
#include <hip/hip_runtime.h>
#include <hip/hip_bf16.h>
#include <stdint.h>

#define GAS __attribute__((address_space(1)))
#define LAS __attribute__((address_space(3)))
typedef unsigned v4u __attribute__((ext_vector_type(4)));
typedef unsigned v2u __attribute__((ext_vector_type(2)));
typedef int v4i __attribute__((ext_vector_type(4)));
typedef float f32x4 __attribute__((ext_vector_type(4)));
typedef float f32x2 __attribute__((ext_vector_type(2)));
typedef short bf16x8 __attribute__((ext_vector_type(8)));
typedef __bf16 bf16x2_t __attribute__((ext_vector_type(2)));

constexpr int NN = 10000, DD = 256, HH = 512, EE = 160000;
constexpr int TN = 40;
constexpr int NT = NN / TN;
constexpr int NC = 64;
constexpr int CH = EE / NC;
constexpr int CAP = 32;
constexpr unsigned SENT = 0xFFFFFFFFu, OVF = 0xFFFFFFFEu, OOBM = 0x80000000u;
static_assert(NT * TN == NN && NC * CH == EE && (CH % 4) == 0, "shape");

constexpr size_t WS_BUCKETS = 0;
constexpr size_t WS_W1T = (size_t)NT * NC * CAP * 4;
constexpr size_t WS_W2T = WS_W1T + (size_t)DD * HH * 2;
constexpr size_t WS_END = WS_W2T + (size_t)DD * HH * 2;

constexpr int PREP_THREADS = 640;
constexpr int PREP_WBLOCKS = (2 * 16384 + PREP_THREADS - 1) / PREP_THREADS;

#define STAMP(buf, idx) do { } while (0)

__device__ __forceinline__ unsigned pack_bf16(float lo, float hi) {
  bf16x2_t v;
  v[0] = (__bf16)lo;
  v[1] = (__bf16)hi;
  return __builtin_bit_cast(unsigned, v);
}
__device__ __forceinline__ float bf_lo(unsigned u) { return __builtin_bit_cast(float, u << 16); }
__device__ __forceinline__ float bf_hi(unsigned u) { return __builtin_bit_cast(float, u & 0xFFFF0000u); }
__device__ __forceinline__ f32x4 mfma16(v4u a, v4u b, f32x4 c) {
  return __builtin_amdgcn_mfma_f32_16x16x32_bf16(__builtin_bit_cast(bf16x8, a), __builtin_bit_cast(bf16x8, b), c, 0, 0, 0);
}

__global__ __launch_bounds__(PREP_THREADS) void k_prep(const int* __restrict__ src, const int* __restrict__ dst,
                                                       const uint16_t* __restrict__ w1, const uint16_t* __restrict__ w2,
                                                       unsigned char* __restrict__ ws
) {
  __shared__ __attribute__((aligned(16))) unsigned bk[NT * CAP + 256];
  const int tid = threadIdx.x;
  const int b = blockIdx.x;
  if (b < NC) {
    unsigned* cnt = bk + NT * CAP;
    v4i d4 = {-1, -1, -1, -1}, s4 = {0, 0, 0, 0};
    if (tid < CH / 4) {
      d4 = *(const v4i*)(dst + b * CH + tid * 4);
      s4 = *(const v4i*)(src + b * CH + tid * 4);
    }
    const v4u sent4 = {SENT, SENT, SENT, SENT};
    for (int i = tid; i < NT * CAP / 4; i += PREP_THREADS) *(v4u*)(bk + i * 4) = sent4;
    if (tid < 256) cnt[tid] = 0;
    __syncthreads();
#pragma unroll
    for (int j = 0; j < 4; ++j) {
      const unsigned d = (unsigned)d4[j];
      if (d < (unsigned)NN) {
        const unsigned t = d / TN, dl = d - t * TN;
        const unsigned r = atomicAdd(&cnt[t], 1u);
        if (r < CAP) bk[t * CAP + ((r + t) & (CAP - 1))] = ((unsigned)s4[j] << 9) | dl;
      }
    }
    __syncthreads();
    v4u* gb = (v4u*)(ws + WS_BUCKETS);
    for (int i = tid; i < NT * 8; i += PREP_THREADS) {
      const int t = i >> 3, q = i & 7;
      v4u v = *(const v4u*)(bk + i * 4);
      if (cnt[t] > CAP) {
        v = sent4;
        if (q == 0) v[0] = OVF;
      }
      gb[(t * NC + b) * 8 + q] = v;
    }
  } else {
    const int g = (b - NC) * PREP_THREADS + tid;
    if (g < 2 * 16384) {
      const int lane = g & 63, i = (g >> 6) & 31, w = (g >> 11) & 7;
      const int m = lane & 15, kg = lane >> 4;
      unsigned short e[8];
      if (g < 16384) {
        const int p = i >> 4, ks = (i >> 1) & 7, cb = i & 1;
        const int hid = 64 * w + 32 * p + 8 * (m >> 2) + 4 * cb + (m & 3);
        const uint16_t* s = w1 + (size_t)(32 * ks + 8 * kg) * HH + hid;
#pragma unroll
        for (int j = 0; j < 8; ++j) e[j] = s[j * HH];
      } else {
        const int ks = i >> 1, cb = i & 1;
        const int col = 32 * w + 8 * (m >> 2) + 4 * cb + (m & 3);
        const uint16_t* s = w2 + (size_t)(32 * ks + 8 * kg) * DD + col;
#pragma unroll
        for (int j = 0; j < 8; ++j) e[j] = s[j * DD];
      }
      v4u v;
#pragma unroll
      for (int j = 0; j < 4; ++j) v[j] = (unsigned)e[2 * j] | ((unsigned)e[2 * j + 1] << 16);
      ((v4u*)(ws + WS_W1T))[g] = v;
    }
  }
}

constexpr int L_HT = 0;
constexpr int L_H1 = 48 * 512;
constexpr int LSTR = 1472;
constexpr int L_LIST = L_H1 + 48 * 1024;
constexpr int L_CNT = L_LIST + 2 * LSTR * 4;
constexpr int L_OVF = L_CNT + 64 * 4;
constexpr int L_END = L_OVF + 80 * 4;
constexpr int RING = 16;

__device__ __forceinline__ int wave_incl_scan(int v) {
  v += __builtin_amdgcn_update_dpp(0, v, 0x111, 0xf, 0xf, false);
  v += __builtin_amdgcn_update_dpp(0, v, 0x112, 0xf, 0xf, false);
  v += __builtin_amdgcn_update_dpp(0, v, 0x114, 0xf, 0xf, false);
  v += __builtin_amdgcn_update_dpp(0, v, 0x118, 0xf, 0xf, false);
  v += __builtin_amdgcn_update_dpp(0, v, 0x142, 0xa, 0xf, false);
  v += __builtin_amdgcn_update_dpp(0, v, 0x143, 0xc, 0xf, false);
  return v;
}

__device__ __forceinline__ void acc_row(float (&acc)[8], v4u v, float sc) {
#pragma unroll
  for (int j = 0; j < 4; ++j) {
    acc[2 * j] = __builtin_fmaf(bf_lo(v[j]), sc, acc[2 * j]);
    acc[2 * j + 1] = __builtin_fmaf(bf_hi(v[j]), sc, acc[2 * j + 1]);
  }
}
__device__ __forceinline__ void fold_halves(const float (&acc)[8], float (&s)[4]) {
#pragma unroll
  for (int i = 0; i < 4; ++i) {
    auto r = __builtin_amdgcn_permlane32_swap(__builtin_bit_cast(unsigned, acc[i]), __builtin_bit_cast(unsigned, acc[i + 4]), false, false);
    const unsigned r0 = r[0], r1 = r[1];
    s[i] = __builtin_bit_cast(float, r0) + __builtin_bit_cast(float, r1);
  }
}

__global__ __launch_bounds__(512, 2) void k_main(const uint16_t* __restrict__ x, const int* __restrict__ src,
                                                 const int* __restrict__ dst, const uint16_t* __restrict__ epsp,
                                                 const uint16_t* __restrict__ b1, const uint16_t* __restrict__ b2,
                                                 const unsigned char* ws, uint16_t* __restrict__ out
) {
  extern __shared__ __attribute__((aligned(16))) unsigned char smem[];
  LAS unsigned char* L = (LAS unsigned char*)smem;
  LAS unsigned* list = (LAS unsigned*)(L + L_LIST);
  LAS unsigned* cnt = (LAS unsigned*)(L + L_CNT);
  LAS unsigned* ovf = (LAS unsigned*)(L + L_OVF);
  const int tid = threadIdx.x, lane = tid & 63, w = __builtin_amdgcn_readfirstlane(tid >> 6);
  const int tile = blockIdx.x;
  const int half = lane >> 5, m16 = lane & 15, g4 = lane >> 4;

  const GAS v4u* gb = (const GAS v4u*)(ws + WS_BUCKETS);
  const GAS v4u* w1t = (const GAS v4u*)(ws + WS_W1T) + w * 32 * 64 + lane;
  const GAS v4u* w2t = (const GAS v4u*)(ws + WS_W2T) + w * 32 * 64 + lane;
  const v4u bkt = gb[tile * 512 + tid];
  const unsigned epsraw = *epsp;
  v4u bias1[2];
  bias1[0] = *(const GAS v4u*)(b1 + 64 * w + 8 * g4);
  bias1[1] = *(const GAS v4u*)(b1 + 64 * w + 32 + 8 * g4);
  const v4u bias2 = *(const GAS v4u*)(b2 + 32 * w + 8 * g4);
  __builtin_amdgcn_sched_barrier(0);
  v4u wf[32];
#pragma unroll
  for (int i = 0; i < 32; ++i) wf[i] = w1t[i * 64];
  __builtin_amdgcn_sched_barrier(0);

  if (tid < 64) cnt[tid] = 1;
  if (tid == 64) ovf[0] = 0;
  __syncthreads();
  unsigned rk[4];
#pragma unroll
  for (int j = 0; j < 4; ++j) {
    const unsigned e = bkt[j];
    rk[j] = 0;
    if (e < OVF) rk[j] = __hip_atomic_fetch_add(&cnt[e & 63], 1u, __ATOMIC_RELAXED, __HIP_MEMORY_SCOPE_WORKGROUP);
    else if (e == OVF) {
      const unsigned o = __hip_atomic_fetch_add(&ovf[0], 1u, __ATOMIC_RELAXED, __HIP_MEMORY_SCOPE_WORKGROUP);
      ovf[1 + o] = tid >> 3;
    }
  }
  __syncthreads();
  STAMP(st, 1);
  int cn = lane < TN ? (int)cnt[lane] : 0;
  const int wpad = (lane / 5) * RING;
  int pend = wave_incl_scan((cn + 1) >> 1) + wpad;
  int pstart = pend - ((cn + 1) >> 1);
#pragma unroll
  for (int j = 0; j < 4; ++j) {
    const unsigned e = bkt[j];
    const int ps = __shfl(pstart, (int)(e & 63), 64);
    if (e < OVF) list[(rk[j] & 1) * LSTR + ps + (rk[j] >> 1)] = e & ~511u;
  }
  const int pbeg = __builtin_amdgcn_readlane(pstart, 5 * w);
  const int pfin = __builtin_amdgcn_readlane(pend, 5 * w + 4);
  if ((lane & 31) < RING) list[half * LSTR + pfin + (lane & 31)] = OOBM;
  if (w == 0 && lane < TN) {
    list[pstart] = (unsigned)(tile * TN + lane) << 9;
    if (cn & 1) list[LSTR + pend - 1] = OOBM;
  }
  const int novf = __builtin_amdgcn_readfirstlane((int)ovf[0]);
  __syncthreads();
  STAMP(st, 2);

  const float cself = __builtin_bit_cast(float, pack_bf16(1.0f + bf_lo(epsraw), 0.f) << 16);
  const float sc_first = half == 0 ? cself : 1.0f;
  const __amdgpu_buffer_rsrc_t xr = __builtin_amdgcn_make_buffer_rsrc((void*)x, 0, NN * DD * 2, 0x00020000);
  const unsigned lane_off = (lane & 31) * 16;

  if (novf == 0) {
    const LAS unsigned* lp = list + half * LSTR + pbeg;
    v4u buf[RING];
    unsigned a[RING];
#pragma unroll
    for (int s = 0; s < RING; ++s) a[s] = lp[s];
#pragma unroll
    for (int s = 0; s < RING; ++s) {
      buf[s] = __builtin_amdgcn_raw_buffer_load_b128(xr, a[s] + lane_off, 0, 0);
      __builtin_amdgcn_sched_barrier(0);
    }
    int node = 5 * w;
    int rem = __builtin_amdgcn_readlane(pend, node) - pbeg;
    const int pcv = (cn + 1) >> 1;
    float acc[8];
#pragma unroll
    for (int i = 0; i < 8; ++i) acc[i] = 0.f;
    float sc = sc_first;
    for (;;) {
      lp += RING;
#pragma unroll
      for (int s = 0; s < RING; ++s) a[s] = lp[s];
#pragma unroll
      for (int s = 0; s < RING; ++s) {
        acc_row(acc, buf[s], sc);
        sc = 1.0f;
        buf[s] = __builtin_amdgcn_raw_buffer_load_b128(xr, a[s] + lane_off, 0, 0);
        --rem;
        if (__builtin_expect(rem == 0, 0)) {
          float sm[4];
          fold_halves(acc, sm);
          v2u o;
          o[0] = pack_bf16(sm[0], sm[1]);
          o[1] = pack_bf16(sm[2], sm[3]);
          *(LAS v2u*)(L + L_HT + node * 512 + (((lane & 31) ^ (node & 15)) << 4) + half * 8) = o;
#pragma unroll
          for (int i = 0; i < 8; ++i) acc[i] = 0.f;
          sc = sc_first;
          ++node;
          if (node == 5 * w + 5) goto gather_done;
          rem = __builtin_amdgcn_readlane(pcv, node);
        }
      }
    }
  gather_done:;
  } else {
    LAS float* accl = (LAS float*)(L + L_H1);
    for (int round = 0; round <= novf; ++round) {
      if (round > 0) {
        const int c = (int)ovf[round];
        __syncthreads();
        if (tid < 64) cnt[tid] = 0;
        __syncthreads();
        unsigned ee[5], rr[5];
#pragma unroll
        for (int j = 0; j < 5; ++j) {
          const int ei = tid + j * 512;
          ee[j] = SENT;
          rr[j] = 0;
          if (ei < CH) {
            const unsigned dl = (unsigned)dst[c * CH + ei] - (unsigned)(tile * TN);
            if (dl < (unsigned)TN) {
              ee[j] = ((unsigned)src[c * CH + ei] << 9) | dl;
              rr[j] = __hip_atomic_fetch_add(&cnt[dl], 1u, __ATOMIC_RELAXED, __HIP_MEMORY_SCOPE_WORKGROUP);
            }
          }
        }
        __syncthreads();
        cn = lane < TN ? (int)cnt[lane] : 0;
        pend = wave_incl_scan((cn + 1) >> 1) + wpad;
        pstart = pend - ((cn + 1) >> 1);
#pragma unroll
        for (int j = 0; j < 5; ++j) {
          const unsigned e = ee[j];
          const int ps = __shfl(pstart, (int)(e & 63), 64);
          if (e < OVF) list[(rr[j] & 1) * LSTR + ps + (rr[j] >> 1)] = e & ~511u;
        }
        if (w == 0 && lane < TN && (cn & 1)) list[LSTR + pend - 1] = OOBM;
        __syncthreads();
      }
      for (int j = 0; j < 5; ++j) {
        const int node = 5 * w + j;
        const int p0 = __builtin_amdgcn_readlane(pstart, node), p1 = __builtin_amdgcn_readlane(pend, node);
        float acc[8];
#pragma unroll
        for (int i = 0; i < 8; ++i) acc[i] = 0.f;
        float sc = round == 0 ? sc_first : 1.0f;
        for (int p = p0; p < p1; ++p) {
          const v4u v = __builtin_amdgcn_raw_buffer_load_b128(xr, list[half * LSTR + p] + lane_off, 0, 0);
          acc_row(acc, v, sc);
          sc = 1.0f;
        }
        float sm[4];
        fold_halves(acc, sm);
        LAS f32x4* ap = (LAS f32x4*)(accl + node * 256 + (lane & 31) * 8 + half * 4);
        f32x4 cur = {sm[0], sm[1], sm[2], sm[3]};
        if (round > 0) cur += *ap;
        *ap = cur;
        if (round == novf) {
          v2u o;
          o[0] = pack_bf16(cur[0], cur[1]);
          o[1] = pack_bf16(cur[2], cur[3]);
          *(LAS v2u*)(L + L_HT + node * 512 + (((lane & 31) ^ (node & 15)) << 4) + half * 8) = o;
        }
      }
    }
  }
  STAMP(st, 3);
  __syncthreads();
  STAMP(st, 4);

#pragma unroll
  for (int p = 0; p < 2; ++p) {
    f32x4 acc[2][3];
#pragma unroll
    for (int cb = 0; cb < 2; ++cb) {
      const f32x4 bv = {bf_lo(bias1[p][2 * cb]), bf_hi(bias1[p][2 * cb]), bf_lo(bias1[p][2 * cb + 1]), bf_hi(bias1[p][2 * cb + 1])};
#pragma unroll
      for (int rb = 0; rb < 3; ++rb) acc[cb][rb] = bv;
    }
#pragma unroll
    for (int ks = 0; ks < 8; ++ks) {
      v4u hb[3];
#pragma unroll
      for (int rb = 0; rb < 3; ++rb)
        hb[rb] = *(const LAS v4u*)(L + L_HT + (16 * rb + m16) * 512 + (((4 * ks + g4) ^ m16) << 4));
#pragma unroll
      for (int cb = 0; cb < 2; ++cb) {
        const int i = p * 16 + ks * 2 + cb;
#pragma unroll
        for (int rb = 0; rb < 3; ++rb) acc[cb][rb] = mfma16(wf[i], hb[rb], acc[cb][rb]);
        wf[i] = w2t[i * 64];
      }
    }
#pragma unroll
    for (int rb = 0; rb < 3; ++rb) {
      float v[8];
#pragma unroll
      for (int cb = 0; cb < 2; ++cb)
#pragma unroll
        for (int i = 0; i < 4; ++i) {
          const float t = acc[cb][rb][i];
          v[cb * 4 + i] = t > 0.f ? t : 0.f * t;
        }
      v4u o;
#pragma unroll
      for (int j = 0; j < 4; ++j) o[j] = pack_bf16(v[2 * j], v[2 * j + 1]);
      *(LAS v4u*)(L + L_H1 + (16 * rb + m16) * 1024 + (((8 * w + 4 * p + g4) ^ m16) << 4)) = o;
    }
  }
  STAMP(st, 5);
  __syncthreads();

  {
    f32x4 acc[2][3];
#pragma unroll
    for (int cb = 0; cb < 2; ++cb) {
      const f32x4 bv = {bf_lo(bias2[2 * cb]), bf_hi(bias2[2 * cb]), bf_lo(bias2[2 * cb + 1]), bf_hi(bias2[2 * cb + 1])};
#pragma unroll
      for (int rb = 0; rb < 3; ++rb) acc[cb][rb] = bv;
    }
#pragma unroll
    for (int ks = 0; ks < 16; ++ks) {
      v4u hb[3];
#pragma unroll
      for (int rb = 0; rb < 3; ++rb)
        hb[rb] = *(const LAS v4u*)(L + L_H1 + (16 * rb + m16) * 1024 + (((4 * ks + g4) ^ m16) << 4));
#pragma unroll
      for (int cb = 0; cb < 2; ++cb)
#pragma unroll
        for (int rb = 0; rb < 3; ++rb) acc[cb][rb] = mfma16(wf[ks * 2 + cb], hb[rb], acc[cb][rb]);
    }
#pragma unroll
    for (int rb = 0; rb < 3; ++rb) {
      v4u o;
      o[0] = pack_bf16(acc[0][rb][0], acc[0][rb][1]);
      o[1] = pack_bf16(acc[0][rb][2], acc[0][rb][3]);
      o[2] = pack_bf16(acc[1][rb][0], acc[1][rb][1]);
      o[3] = pack_bf16(acc[1][rb][2], acc[1][rb][3]);
      const int r = 16 * rb + m16;
      if (r < TN) *(GAS v4u*)(out + (size_t)(tile * TN + r) * DD + 32 * w + 8 * g4) = o;
    }
  }
  STAMP(st, 6);
}

extern "C" void kernel_launch(void* const* d_in, const int* in_sizes, int n_in, void* d_out, int out_size, void* d_ws,
                              size_t ws_size, hipStream_t stream) {
  const uint16_t* x = (const uint16_t*)d_in[0];
  const int* src = (const int*)d_in[1];
  const int* dst = (const int*)d_in[2];
  const uint16_t* eps = (const uint16_t*)d_in[3];
  const uint16_t* w1 = (const uint16_t*)d_in[4];
  const uint16_t* b1 = (const uint16_t*)d_in[5];
  const uint16_t* w2 = (const uint16_t*)d_in[6];
  const uint16_t* b2 = (const uint16_t*)d_in[7];
  unsigned char* ws = (unsigned char*)d_ws;
  (void)hipFuncSetAttribute((const void*)k_main, hipFuncAttributeMaxDynamicSharedMemorySize, L_END);
  k_prep<<<NC + PREP_WBLOCKS, PREP_THREADS, 0, stream>>>(src, dst, w1, w2, ws);
  k_main<<<NT, 512, L_END, stream>>>(x, src, dst, eps, b1, b2, ws, (uint16_t*)d_out);
}
```

```cpp
#include <hip/hip_runtime.h>
#include <hip/hip_bf16.h>
#include <stdint.h>

#define GAS __attribute__((address_space(1)))
#define LAS __attribute__((address_space(3)))
typedef unsigned v4u __attribute__((ext_vector_type(4)));
typedef unsigned v2u __attribute__((ext_vector_type(2)));
typedef unsigned v32u __attribute__((ext_vector_type(32)));
typedef int v4i __attribute__((ext_vector_type(4)));
typedef float f32x4 __attribute__((ext_vector_type(4)));
typedef __bf16 bf16x2_t __attribute__((ext_vector_type(2)));
#define RLX_WG __ATOMIC_RELAXED, __HIP_MEMORY_SCOPE_WORKGROUP
#define RLX_AGENT __ATOMIC_RELAXED, __HIP_MEMORY_SCOPE_AGENT

constexpr int NN = 10000, DD = 256, HH = 512, EE = 160000;
constexpr int TN = 40;
constexpr int NT = NN / TN;
constexpr int NC = 64;
constexpr int CH = EE / NC;
constexpr int CAP = 32;
constexpr unsigned E_NONE = 63, E_OVF = 62, OOBM = 0x80000000u;
static_assert(NT * TN == NN && NC * CH == EE && (CH % 4) == 0, "shape");

constexpr int WS_EPOCH = 0;
constexpr int WS_FLAG = 4096;
constexpr int WS_INBOX = 16384;
constexpr int WS_WT = WS_INBOX + NT * NC * 128;
constexpr int WS_END = WS_WT + 2 * DD * HH * 2;

constexpr unsigned long long T_POLL = 210;
constexpr unsigned long long SDELAY = 40;
constexpr unsigned long long T_GIVEUP = 20000;
constexpr int BACKOFF = 10;
constexpr int XW = 4;

constexpr int L_HT = 0;
constexpr int L_H1 = 48 * 512;
constexpr int L_LIST = L_H1 + 48 * 1024;
constexpr int LIST_WORDS = 2944;
constexpr int L_CNT = L_LIST + LIST_WORDS * 4;
constexpr int L_OVF = L_CNT + 64 * 4;
constexpr int L_BIAS = L_OVF + 128 * 4;
constexpr int L_END = L_BIAS + 2048;
constexpr int RING = 16;

__device__ __forceinline__ unsigned pack_bf16(float lo, float hi) {
  bf16x2_t v;
  v[0] = (__bf16)lo;
  v[1] = (__bf16)hi;
  return __builtin_bit_cast(unsigned, v);
}
__device__ __forceinline__ float bf_lo(unsigned u) { return __builtin_bit_cast(float, u << 16); }
__device__ __forceinline__ float bf_hi(unsigned u) { return __builtin_bit_cast(float, u & 0xFFFF0000u); }
__device__ __forceinline__ unsigned long long clock100() { return __builtin_amdgcn_s_memrealtime(); }
__device__ __forceinline__ int wave_incl_scan(int v) {
  v += __builtin_amdgcn_update_dpp(0, v, 0x111, 0xf, 0xf, false);
  v += __builtin_amdgcn_update_dpp(0, v, 0x112, 0xf, 0xf, false);
  v += __builtin_amdgcn_update_dpp(0, v, 0x114, 0xf, 0xf, false);
  v += __builtin_amdgcn_update_dpp(0, v, 0x118, 0xf, 0xf, false);
  v += __builtin_amdgcn_update_dpp(0, v, 0x142, 0xa, 0xf, false);
  v += __builtin_amdgcn_update_dpp(0, v, 0x143, 0xc, 0xf, false);
  return v;
}
__device__ __forceinline__ v4u make_srd(const void* p, unsigned bytes) {
  const unsigned long long a = (unsigned long long)p;
  v4u r;
  r[0] = (unsigned)a; r[1] = (unsigned)(a >> 32) & 0xFFFFu; r[2] = bytes; r[3] = 0x00020000u;
  return r;
}
__device__ __forceinline__ v4u make_fragment(const uint16_t* __restrict__ w1, const uint16_t* __restrict__ w2, int f, int lane) {
  const int i = f & 31, w = (f >> 5) & 7, m = lane & 15, kg = lane >> 4;
  const int sub = 8 * (m >> 2) + 4 * (i & 1) + (m & 3);
  const uint16_t* s = f < 256 ? w1 + (32 * ((i >> 1) & 7) + 8 * kg) * HH + 64 * w + 32 * (i >> 4) + sub
                              : w2 + (32 * (i >> 1) + 8 * kg) * DD + 32 * w + sub;
  const int pitch = f < 256 ? HH : DD;
  unsigned short e[8];
#pragma unroll
  for (int j = 0; j < 8; ++j) e[j] = s[j * pitch];
  v4u v;
#pragma unroll
  for (int j = 0; j < 4; ++j) v[j] = (unsigned)e[2 * j] | ((unsigned)e[2 * j + 1] << 16);
  return v;
}
#define CV10(a) "v" #a "0", "v" #a "1", "v" #a "2", "v" #a "3", "v" #a "4", "v" #a "5", "v" #a "6", "v" #a "7", "v" #a "8", "v" #a "9"

extern "C" __global__ __launch_bounds__(512, 2) void gin_main(unsigned char* ws, const int* __restrict__ src,
                                                              const int* __restrict__ dst, const uint16_t* __restrict__ w1,
                                                              const uint16_t* __restrict__ w2, const uint16_t* __restrict__ x,
                                                              const uint16_t* __restrict__ b1, const uint16_t* __restrict__ b2,
                                                              const uint16_t* __restrict__ epsp, uint16_t* __restrict__ out) {
  extern __shared__ __attribute__((aligned(16))) unsigned char smem[];
  LAS unsigned char* L = (LAS unsigned char*)smem;
  LAS unsigned* cnt = (LAS unsigned*)(L + L_CNT);
  LAS unsigned* ovf = (LAS unsigned*)(L + L_OVF);
  const int tid = threadIdx.x, lane = tid & 63, w = __builtin_amdgcn_readfirstlane(tid >> 6);
  const int tile = blockIdx.x;
  const int half = lane >> 5, m16 = lane & 15, g4 = lane >> 4;
  unsigned cdummy = 0;
  asm volatile("v_cmp_gt_u32 vcc, 144, %1\n s_and_saveexec_b64 s[42:43], vcc\n s_cbranch_execz .Lgin_nc%=\n"
               "s_getpc_b64 s[40:41]\n s_add_u32 s40, s40, gin_main@rel32@lo+4\n s_addc_u32 s41, s41, gin_main@rel32@hi+12\n"
               "v_lshlrev_b32 %0, 7, %1\n global_load_dword %0, %0, s[40:41]\n .Lgin_nc%=:\n s_mov_b64 exec, s[42:43]"
               : "+v"(cdummy) : "v"(tid) : "memory", "scc", "vcc", "s40", "s41", "s42", "s43");
  v4i la, lc;
  int lb, ld;
  unsigned lep;
  asm volatile("s_cmp_ge_u32 %[blk], 64\n s_cbranch_scc1 .Lgin_ne%=\n s_mul_i32 s40, %[blk], 10000\n"
               "v_lshl_add_u32 v48, %[tid], 4, s40\n v_min_u32 v49, 451, %[tid]\n s_addk_i32 s40, 0x2000\n"
               "global_load_dwordx4 v[40:43], v48, %[dst]\n v_lshl_add_u32 v49, v49, 2, s40\n"
               "global_load_dwordx4 v[44:47], v48, %[src]\n global_load_dword v48, v49, %[dst]\n"
               "global_load_dword v49, v49, %[src]\n v_and_b32 v50, 7, %[tid]\n v_lshlrev_b32 v50, 8, v50\n"
               "global_load_dword v50, v50, %[ws] sc1\n .Lgin_ne%=:\n"
               : "=&{v[40:43]}"(la), "=&{v48}"(lb), "=&{v[44:47]}"(lc), "=&{v49}"(ld), "=&{v50}"(lep)
               : [blk] "s"(tile), [tid] "v"(tid), [dst] "s"(dst), [src] "s"(src), [ws] "s"(ws), "s"(epsp), "s"(w1), "s"(w2),
                 "s"(x), "s"(b1), "s"(b2), "s"(out)
               : "memory", "scc", "s40");
  static_assert(WS_EPOCH == 0 && CH * 4 == 10000 && CH - 2048 == 452, "constants of the statement above");
  const unsigned long long tstart = clock100();
  const int grp = tile & 7, gidx = tile >> 3;
  const bool producer = gidx < 8;
  const int ti = gidx < 16 ? tile - 64 : -1;
  const __amdgpu_buffer_rsrc_t wr = __builtin_amdgcn_make_buffer_rsrc((void*)ws, 0, WS_END, 0x00020000);
  const v4u srdx = make_srd(x, NN * DD * 2);
  const unsigned lbase = (unsigned)(size_t)L;
  const unsigned vw = lane * 16;

  unsigned tag;
#define GET_TAG()                                                                                                       \
  do {                                                                                                                  \
    const unsigned ep = __builtin_amdgcn_raw_buffer_load_b32(wr, WS_EPOCH + (lane & 7) * 256, 0, 16);                  \
    tag = 1;                                                                                                            \
    _Pragma("unroll") for (int k = 0; k < 8; ++k) tag += __builtin_amdgcn_readlane(ep, k);                              \
  } while (0)
  unsigned keepm0, xdummy = 0;
  unsigned vs[3];
#pragma unroll
  for (int k = 0; k < 3; ++k) {
    const unsigned r = 5 * w + 2 * k + half;
    vs[k] = (unsigned)(tile * TN + r) * 512 + (((lane & 31) ^ (r & 15)) << 4);
  }
#define OWN_ROWS()                                                                                                      \
  asm volatile("s_mov_b32 %[km], m0\n s_mov_b32 m0, %[l0]\n s_nop 0\n buffer_load_dwordx4 %[a], %[srdx], 0 offen lds\n" \
               "s_add_u32 m0, m0, 0x400\n s_nop 0\n buffer_load_dwordx4 %[b], %[srdx], 0 offen lds\n"                   \
               "s_add_u32 m0, m0, 0x400\n s_mov_b32 exec_hi, 0\n buffer_load_dwordx4 %[c], %[srdx], 0 offen lds\n"      \
               "s_mov_b32 exec_hi, -1\n s_mov_b32 m0, %[km]\n"                                                         \
               : [km] "=&s"(keepm0)                                                                                     \
               : [a] "v"(vs[0]), [b] "v"(vs[1]), [c] "v"(vs[2]), [srdx] "s"(srdx), [l0] "s"(lbase + L_HT + w * 2560)    \
               : "memory", "scc")
#define LDS_CONSTS()                                                                                                    \
  do {                                                                                                                  \
    if (w < 2)                                                                                                          \
      asm volatile("s_mov_b32 %[km], m0\n s_mov_b32 m0, %[lb]\n s_nop 0\n global_load_lds_dwordx4 %[vbo], %[bp]\n"      \
                   "s_mov_b32 m0, %[km]\n"                                                                              \
                   : [km] "=&s"(keepm0)                                                                                 \
                   : [vbo] "v"(w == 0 ? vw : vw & 511u), [bp] "s"(w == 0 ? b1 : b2), [lb] "s"(lbase + L_BIAS + w * 1024) \
                   : "memory");                                                                                         \
    if (w >= 4) *(LAS v4u*)(L + L_HT + TN * 512 + (tid - 256) * 16) = v4u{0u, 0u, 0u, 0u};                             \
  } while (0)
  unsigned vx = grp * 640000 + ((gidx - 16) * XW + w) * 8192 + lane * 128;
  if (vx >= NN * DD * 2) vx -= NN * DD * 2;

  if (__builtin_expect(producer, 1)) {
    const int c = tile;
    LAS unsigned* bk = (LAS unsigned*)(L + L_H1);
    LAS unsigned* bc = bk + 8192;
    const bool five = tid < CH - 2048;
    const v4u none4 = {E_NONE, E_NONE, E_NONE, E_NONE};
#pragma unroll
    for (int k = 0; k < 4; ++k) *(LAS v4u*)(bk + (tid + k * 512) * 4) = none4;
    if (tid < 256) bc[tid] = 0;
    if (tid < 64) cnt[tid] = 0;
    __syncthreads();
    unsigned so0 = WS_INBOX + ((tid >> 3) * NC + c) * 128 + (tid & 7) * 16, so3 = tid < (NT - 192) * 8 ? so0 : OOBM;
    unsigned rd0 = lbase + L_H1 + tid * 16, rn0 = lbase + L_H1 + 32768 + (tid >> 3) * 4;
    unsigned rn3 = lbase + L_H1 + 32768 + min((tid >> 3) + 192, NT) * 4;
    asm volatile("s_waitcnt vmcnt(0)"
                 : "+{v[40:43]}"(la), "+{v48}"(lb), "+{v[44:47]}"(lc), "+{v49}"(ld), "+{v50}"(lep), "+v"(so0),
                   "+v"(so3), "+v"(rd0), "+v"(rn0), "+v"(rn3)::"memory");
    unsigned t4[5], r4[5], en[5];
#pragma unroll
    for (int j = 0; j < 5; ++j) {
      const unsigned d = min((unsigned)(j < 4 ? la[j] : five ? lb : -1), 256u * TN - 1);
      t4[j] = ((d * 52429u) >> 19) & ~3u;
      r4[j] = __hip_atomic_fetch_add((LAS unsigned*)(L + L_H1 + 32768 + t4[j]), 4u, RLX_WG);
      en[j] = ((unsigned)(j < 4 ? lc[j] : ld) << 6 | (d - t4[j] * (TN / 4))) & 0xFFFFFu;
    }
    tag = 1;
#pragma unroll
    for (int k = 0; k < 8; ++k) tag += __builtin_amdgcn_readlane(lep, k);
#pragma unroll
    for (int j = 0; j < 5; ++j)
      *(LAS unsigned*)(L + L_H1 + ((t4[j] << 5) | ((r4[j] + t4[j]) & 124u))) = en[j];
    __syncthreads();
    v4u e[4];
    unsigned n[4];
#pragma unroll
    for (int k = 0; k < 4; ++k) {
      e[k] = *(const LAS v4u*)(size_t)(rd0 + k * 8192);
      n[k] = *(const LAS unsigned*)(size_t)(k < 3 ? rn0 + k * 256 : rn3);
    }
    if (__builtin_expect(__builtin_amdgcn_ballot_w64(max(max(n[0], n[1]), max(n[2], n[3])) > 4 * CAP) != 0, 0)) {
#pragma unroll
      for (int k = 0; k < 4; ++k)
        if (n[k] > 4 * CAP) {
          e[k] = none4;
          if ((tid & 7) == 0) e[k][0] = E_OVF;
        }
    }
    const unsigned t24 = tag & 0xFFFFFFu;
#pragma unroll
    for (int k = 0; k < 4; ++k) {
      v4u v;
      v[0] = t24 | (e[k][0] << 24); v[1] = (e[k][0] >> 8) | (e[k][1] << 12);
      v[2] = t24 | (e[k][2] << 24); v[3] = (e[k][2] >> 8) | (e[k][3] << 12);
      __builtin_amdgcn_raw_buffer_store_b128(v, wr, k < 3 ? so0 : so3, k * (64 * NC * 128), 16);
    }
    OWN_ROWS();
    LDS_CONSTS();
  } else {
    if (tid < 64) cnt[tid] = 0;
    if (ti >= 0) {
      const int f0 = ti * 8 + w;
      const v4u fr = make_fragment(w1, w2, f0, lane);
      __builtin_amdgcn_raw_buffer_store_b128(fr, wr, WS_WT + f0 * 1024 + vw, 0, 16);
      GET_TAG();
      asm volatile("s_waitcnt vmcnt(0)" ::: "memory");
      __syncthreads();
      if (tid == 0) __hip_atomic_store((GAS unsigned*)(ws + WS_FLAG + ti * 128), tag, RLX_AGENT);
    } else {
      GET_TAG();
      __syncthreads();
      while (clock100() - tstart < SDELAY) __builtin_amdgcn_s_sleep(1);
    }
    OWN_ROWS();
    LDS_CONSTS();
    if (ti < 0 && w < XW) asm volatile("buffer_load_dword %0, %1, %2, 0 offen" : "+v"(xdummy) : "v"(vx), "s"(srdx) : "memory");
  }

  const v4u srdw = make_srd(ws + WS_WT, 2 * DD * HH * 2);
  const v4u srdo = make_srd(out, NN * DD * 2);
  const unsigned epsraw = *(const GAS uint16_t*)epsp;
  unsigned lo = (lane & 31) * 16, lo2 = (half * 16 + m16) << 4, hwb = lbase + L_HT + (g4 & 1) * 8;
  const unsigned x0 = (unsigned)(g4 ^ m16) << 4;
  unsigned ha0 = lbase + L_HT + m16 * 512 + x0, hr0 = lbase + L_H1 + m16 * 1024 + x0;
  unsigned h1w = hr0 ^ (w << 7), vb1 = lbase + L_BIAS + 128 * w + 16 * g4;
  unsigned ob = (unsigned)(tile * TN + m16) * 512 + w * 64 + g4 * 16;
  unsigned ob2 = m16 < TN - 32 ? ob + 16384 : OOBM;
  unsigned wpad = lbase + L_LIST + (lane / 5) * (RING * 8);
  v4u aone;
#pragma unroll
  for (int d = 0; d < 4; ++d) {
    const int j = m16 - 8 * (g4 & 1);
    aone[d] = j == 2 * d ? 0x3F80u : j == 2 * d + 1 ? 0x3F800000u : 0u;
  }
  unsigned vwp = vw, dum = 512 + lane * 4;
  asm volatile("" : "+{v1}"(lo), "+{v2}"(lo2), "+{v3}"(hwb), "+{v4}"(ha0), "+{v5}"(hr0), "+{v6}"(h1w), "+{v7}"(vb1), "+{v8}"(ob),
               "+{v9}"(ob2), "+{v10}"(vwp), "+v"(wpad), "+v"(dum), "+{v[32:35]}"(aone));

  const int fq = (lane & 4) * 8 + 4 * w + (lane & 3), foff = WS_FLAG + fq * 128;
  unsigned fl = 0, d4[4], val[4], rk[4];
  int cn, cb, pend, pstart;
  v4u g;
  {
    while (clock100() - tstart < T_POLL) __builtin_amdgcn_s_sleep(2);
    bool ok = false;
    for (;;) {
      if (!ok) g = __builtin_amdgcn_raw_buffer_load_b128(wr, WS_INBOX + tile * (NC * 128) + tid * 16, 0, 16);
      ok = (((g[0] ^ tag) | (g[2] ^ tag)) & 0xFFFFFFu) == 0;
      const unsigned long long miss = __builtin_amdgcn_ballot_w64(!ok);
      if (miss == 0) break;
      if (clock100() - tstart > T_GIVEUP) {
        if ((miss >> (lane & 56)) & 0xFF) {
          g = v4u{E_NONE << 24, E_NONE << 12, E_NONE << 24, E_NONE << 12};
          if ((lane & 7) == 0) g[0] = E_OVF << 24;
        }
        break;
      }
      __builtin_amdgcn_s_sleep(BACKOFF);
    }
    asm volatile("s_waitcnt vmcnt(0)" : "+v"(xdummy), "+v"(cdummy)::"memory");
  }
  d4[0] = (g[0] >> 22) & 0xFC;  d4[1] = (g[1] >> 10) & 0xFC;  d4[2] = (g[2] >> 22) & 0xFC;  d4[3] = (g[3] >> 10) & 0xFC;
#pragma unroll
  for (int j = 0; j < 4; ++j)
    rk[j] = __hip_atomic_fetch_add((LAS unsigned*)(L + L_CNT + (d4[j] < TN * 4 ? d4[j] : dum)), 4u, RLX_WG);
  val[0] = (__builtin_amdgcn_alignbit(g[1], g[0], 30) & 0x3FFFu) << 9;  val[1] = (g[1] >> 18) << 9;
  val[2] = (__builtin_amdgcn_alignbit(g[3], g[2], 30) & 0x3FFFu) << 9;  val[3] = (g[3] >> 18) << 9;
  if (__builtin_expect(__builtin_amdgcn_ballot_w64(d4[0] == E_OVF * 4) != 0, 0)) {
    if (d4[0] == E_OVF * 4) ovf[__hip_atomic_fetch_add(&cnt[63], 1u, RLX_WG)] = tid >> 3;
  }
  __syncthreads();
  cn = (int)cnt[lane];
  cb = lane < TN ? max(8, (cn + 4) & ~7) : 0;
  pend = wave_incl_scan(cb) + (int)wpad;
  pstart = pend - cb;
  {
    int ps[4];
#pragma unroll
    for (int j = 0; j < 4; ++j) ps[j] = __builtin_amdgcn_ds_bpermute((int)d4[j], pstart);
    const int pfin = __builtin_amdgcn_readlane(pend, 5 * w + 4);
    if (lane < 2 * RING) *(LAS unsigned*)(size_t)(unsigned)(pfin + lane * 4) = OOBM;
    if (lane < TN && cn != cb && lane % 8 == w) {
      *(LAS unsigned*)(size_t)(unsigned)(pend - 4) = OOBM;
      if (cn == 0) *(LAS unsigned*)(size_t)(unsigned)pstart = OOBM;
    }
#pragma unroll
    for (int j = 0; j < 4; ++j) *(LAS unsigned*)(size_t)(d4[j] < TN * 4 ? ps[j] + rk[j] : lbase + L_CNT + dum) = val[j];
  }
  const float cself = __builtin_bit_cast(float, pack_bf16(1.0f + bf_lo(epsraw), 0.f) << 16);
  const int cs = __builtin_amdgcn_readfirstlane(__builtin_bit_cast(int, cself));
  const int novf = __builtin_amdgcn_readlane(cn, 63);
  const int pbeg = __builtin_amdgcn_readlane(pstart, 5 * w);
  unsigned lp = pbeg + half * 4;
  const int extra = (max(((__builtin_amdgcn_readlane(pend, 5 * w + 4) - pbeg) >> 3) - RING, 0) + 3) & ~3;
  unsigned rounds = (extra + 15) >> 4, ent = (-extra & 15) >> 2;
  const int pcm1 = (cb >> 3) - 1;
  unsigned node = 5 * w, rem = __builtin_amdgcn_readlane(pcm1, 5 * w);
  int wearly = 0;
  __syncthreads();

  if (__builtin_expect(novf != 0, 0)) {
    const __amdgpu_buffer_rsrc_t xr = __builtin_amdgcn_make_buffer_rsrc((void*)x, 0, NN * DD * 2, 0x00020000);
    LAS float* accl = (LAS float*)(L + L_H1);
    for (int round = 0; round <= novf; ++round) {
      if (round > 0) {
        const int c = (int)ovf[round - 1];
        __syncthreads();
        if (tid < TN) cnt[tid] = 0;
        __syncthreads();
        unsigned dd[5], vv[5], rr[5];
#pragma unroll
        for (int j = 0; j < 5; ++j) {
          const int ei = tid + j * 512;
          dd[j] = E_NONE;
          vv[j] = rr[j] = 0;
          if (ei < CH) {
            const unsigned dl = (unsigned)dst[c * CH + ei] - (unsigned)(tile * TN);
            if (dl < (unsigned)TN) {
              dd[j] = dl;
              vv[j] = min((unsigned)src[c * CH + ei], 16383u) << 9;
              rr[j] = __hip_atomic_fetch_add(&cnt[dl], 4u, RLX_WG);
            }
          }
        }
        __syncthreads();
        cn = (int)cnt[lane];
        cb = lane < TN ? (cn + 4) & ~7 : 0;
        pend = wave_incl_scan(cb) + (int)wpad;
        pstart = pend - cb;
#pragma unroll
        for (int j = 0; j < 5; ++j) {
          const int ps = __builtin_amdgcn_ds_bpermute((int)(dd[j] << 2), pstart);
          if (dd[j] < (unsigned)TN) *(LAS unsigned*)(size_t)(unsigned)(ps + rr[j]) = vv[j];
        }
        if (w == 0 && lane < TN && cn != cb) *(LAS unsigned*)(size_t)(unsigned)(pend - 4) = OOBM;
        __syncthreads();
      }
      for (int j = 0; j < 5; ++j) {
        const int row = 5 * w + j;
        const int p0 = __builtin_amdgcn_readlane(pstart, row), p1 = __builtin_amdgcn_readlane(pend, row);
        float acc[8];
#pragma unroll
        for (int i = 0; i < 8; ++i) acc[i] = 0.f;
        for (int p = p0; p < p1; p += 32) {
          v4u v[4];
#pragma unroll
          for (int u = 0; u < 4; ++u) {
            const unsigned o = p + u * 8 < p1 ? *(LAS unsigned*)(size_t)(unsigned)(p + u * 8 + half * 4) : OOBM;
            v[u] = __builtin_amdgcn_raw_buffer_load_b128(xr, o + lo, 0, 0);
          }
#pragma unroll
          for (int u = 0; u < 4; ++u)
#pragma unroll
            for (int i = 0; i < 4; ++i) {
              acc[2 * i] += bf_lo(v[u][i]);
              acc[2 * i + 1] += bf_hi(v[u][i]);
            }
        }
        f32x4 cur;
#pragma unroll
        for (int i = 0; i < 4; ++i) {
          auto sw = __builtin_amdgcn_permlane32_swap(__builtin_bit_cast(unsigned, acc[i]), __builtin_bit_cast(unsigned, acc[i + 4]), false, false);
          const unsigned s0 = sw[0], s1 = sw[1];
          cur[i] = __builtin_bit_cast(float, s0) + __builtin_bit_cast(float, s1);
        }
        LAS f32x4* ap = (LAS f32x4*)(accl + row * 256 + (lane & 31) * 8 + half * 4);
        if (round > 0) cur += *ap;
        *ap = cur;
        if (round == novf) {
          LAS v2u* hp = (LAS v2u*)(L + L_HT + row * 512 + (((lane & 31) ^ (row & 15)) << 4) + half * 8);
          const v2u own = *(const GAS v2u*)(x + (size_t)(tile * TN + row) * DD + (lane & 31) * 8 + half * 4);
          v2u o;
          o[0] = pack_bf16(__builtin_fmaf(bf_lo(own[0]), cself, cur[0]), __builtin_fmaf(bf_hi(own[0]), cself, cur[1]));
          o[1] = pack_bf16(__builtin_fmaf(bf_lo(own[1]), cself, cur[2]), __builtin_fmaf(bf_hi(own[1]), cself, cur[3]));
          *hp = o;
        }
      }
    }
  }

  unsigned soffw = w * 32768;
  v32u wq0, wq1, wq2, wq3;
  if (__builtin_expect(novf == 0, 1)) {
    asm volatile(R"ASM(
.macro GACC
  s_sub_u32 %[rem], %[rem], 1
  s_getpc_b64 s[42:43]
  s_cbranch_scc1 .Lgin_flush%=
.endm
; h tile address of node %[node] -> v42, own row -> v[40:41], sums cleared
.macro NEXTN
  s_and_b32 s46, %[node], 15
  s_lshl_b32 s46, s46, 4
  s_lshl_b32 s47, %[node], 9
  v_xor_b32 v42, s46, %[lo2]
  v_add3_u32 v42, v42, s47, %[hwb]
  v_mov_b32 v36, 0
  ds_read_b64 v[40:41], v42
  v_mov_b32 v37, 0
  v_mov_b32 v38, 0
  v_mov_b32 v39, 0
.endm
; request the pair at list position t into ring slot t mod 16, fetch the address of position t + 4
.macro FILL t
  v_add_u32 v[56+(\t&3)], v[56+(\t&3)], %[lo]
  buffer_load_dwordx4 v[64+4*(\t&15):64+4*(\t&15)+3], v[56+(\t&3)], %[srdx], 0 offen
  ds_read_b32 v[56+(\t&3)], %[lp] offset:(\t+4)*8
.endm
.macro FILL4 t
  s_waitcnt lgkmcnt(3)
  FILL \t
  s_waitcnt lgkmcnt(3)
  FILL (\t+1)
  s_waitcnt lgkmcnt(3)
  FILL (\t+2)
  s_waitcnt lgkmcnt(3)
  FILL (\t+3)
  s_sub_u32 s46, s46, 1
  s_cbranch_scc1 .Lgin_filled%=
.endm
; one gather slot: one MFMA adds the two rows in ring slot i; the slot is refilled
.macro GSLOT i
  s_waitcnt vmcnt(15) lgkmcnt(3)
  v_mfma_f32_16x16x32_bf16 v[36:39], v[32:35], v[64+4*\i:64+4*\i+3], v[36:39]
  FILL (\i+16)
  GACC
.endm
; the last RING slots request weight fragments instead
.macro DSLOT i
  s_waitcnt vmcnt(15)
  v_mfma_f32_16x16x32_bf16 v[36:39], v[32:35], v[64+4*\i:64+4*\i+3], v[36:39]
  WL (128+4*\i)
  GACC
.endm
; the same without requests, when the weights were not seen published: the wait count falls by one per slot
.macro PSLOT i
  s_waitcnt vmcnt(15-\i)
  v_mfma_f32_16x16x32_bf16 v[36:39], v[32:35], v[64+4*\i:64+4*\i+3], v[36:39]
  GACC
.endm
.macro WL r
  buffer_load_dwordx4 v[\r:\r+3], %[vw], %[srdw], %[soffw] offen sc1
  s_addk_i32 %[soffw], 0x400
.endm
  s_mov_b32 s40, 0xffff0000
  ds_read_b32 v56, %[lp]
  ds_read_b32 v57, %[lp] offset:8
  ds_read_b32 v58, %[lp] offset:16
  ds_read_b32 v59, %[lp] offset:24
  buffer_load_dword v14, %[foff], %[srdf], 0 offen sc1
  s_setprio 3
  s_lshl_b32 s46, %[ent], 5
  v_subrev_u32 %[lp], s46, %[lp]
  s_getpc_b64 s[48:49]
.Lgin_pc%=:
  s_mul_i32 s46, %[ent], .Lgin_f1%=-.Lgin_f0%=
  s_mul_i32 s47, %[ent], .Lgin_l1%=-.Lgin_loop%=
  s_add_u32 s44, s48, s46
  s_addc_u32 s45, s49, 0
  s_add_u32 s44, s44, .Lgin_f0%=-.Lgin_pc%=
  s_addc_u32 s45, s45, 0
  s_add_u32 s48, s48, s47
  s_addc_u32 s49, s49, 0
  s_add_u32 s48, s48, .Lgin_loop%=-.Lgin_pc%=
  s_addc_u32 s49, s49, 0
  s_mov_b32 s46, 3
  s_setpc_b64 s[44:45]
.Lgin_f0%=:
  FILL4 0
.Lgin_f1%=:
  FILL4 4
  FILL4 8
  FILL4 12
  FILL4 16
  FILL4 20
  FILL4 24
.Lgin_filled%=:
  s_waitcnt vmcnt(16)
  NEXTN
  s_sub_u32 %[rounds], %[rounds], 1
  s_cbranch_scc1 .Lgin_drain%=
  s_setpc_b64 s[48:49]
.Lgin_loop%=:
  GSLOT 0
  GSLOT 1
  GSLOT 2
  GSLOT 3
.Lgin_l1%=:
  GSLOT 4
  GSLOT 5
  GSLOT 6
  GSLOT 7
  GSLOT 8
  GSLOT 9
  GSLOT 10
  GSLOT 11
  GSLOT 12
  GSLOT 13
  GSLOT 14
  GSLOT 15
  v_add_u32 %[lp], 0x80, %[lp]
  s_sub_u32 %[rounds], %[rounds], 1
  s_cbranch_scc0 .Lgin_loop%=
.Lgin_drain%=:
  ; weight fragments may be requested once the flags of their eight tilers were seen
  v_cmp_ne_u32 vcc, %[tag], v14
  s_cbranch_vccnz .Lgin_plain%=
  s_mov_b32 %[wearly], 1
  DSLOT 0
  DSLOT 1
  DSLOT 2
  DSLOT 3
  DSLOT 4
  DSLOT 5
  DSLOT 6
  DSLOT 7
  DSLOT 8
  DSLOT 9
  DSLOT 10
  DSLOT 11
  DSLOT 12
  DSLOT 13
  DSLOT 14
  DSLOT 15
  s_branch .Lgin_done%=
.Lgin_plain%=:
  PSLOT 0
  PSLOT 1
  PSLOT 2
  PSLOT 3
  PSLOT 4
  PSLOT 5
  PSLOT 6
  PSLOT 7
  PSLOT 8
  PSLOT 9
  PSLOT 10
  PSLOT 11
  PSLOT 12
  PSLOT 13
  PSLOT 14
  PSLOT 15
  s_waitcnt vmcnt(0)
  s_branch .Lgin_done%=
.Lgin_flush%=:
  s_add_u32 %[node], %[node], 1
  s_waitcnt lgkmcnt(0)
  v_lshlrev_b32 v44, 16, v40
  v_and_b32 v45, s40, v40
  v_lshlrev_b32 v46, 16, v41
  v_and_b32 v47, s40, v41
  v_fmac_f32 v36, %[cs], v44
  v_fmac_f32 v37, %[cs], v45
  v_fmac_f32 v38, %[cs], v46
  v_fmac_f32 v39, %[cs], v47
  v_cvt_pk_bf16_f32 v44, v36, v37
  v_cvt_pk_bf16_f32 v45, v38, v39
  s_sub_u32 s46, %[nlast], %[node]
  ds_write_b64 v42, v[44:45]
  s_cbranch_scc1 .Lgin_last%=
  v_readlane_b32 %[rem], %[pcm1], %[node]
  ; the further ahead a wave is, the lower its priority
  s_cmp_eq_u32 s46, 2
  s_cbranch_scc0 .Lgin_p1%=
  s_setprio 2
  buffer_load_dword v14, %[foff], %[srdf], 0 offen sc1
.Lgin_p1%=:
  s_cmp_eq_u32 s46, 1
  s_cbranch_scc0 .Lgin_p2%=
  s_setprio 1
.Lgin_p2%=:
  s_cmp_eq_u32 s46, 0
  s_cbranch_scc0 .Lgin_p3%=
  s_setprio 0
.Lgin_p3%=:
  NEXTN
.Lgin_ret%=:
  s_add_u32 s42, s42, 4
  s_addc_u32 s43, s43, 0
  s_setpc_b64 s[42:43]

.Lgin_last%=:
  s_brev_b32 %[rem], -2
  s_branch .Lgin_ret%=
.Lgin_done%=:
  s_waitcnt lgkmcnt(0)
.purgem GACC
.purgem NEXTN
.purgem FILL
.purgem FILL4
.purgem GSLOT
.purgem DSLOT
.purgem PSLOT
.purgem WL
)ASM"
                 : [lp] "+v"(lp), [node] "+s"(node), [rem] "+s"(rem), [rounds] "+s"(rounds), [soffw] "+s"(soffw), [wearly] "+s"(wearly),
                   "=&{v14}"(fl), "=&{v[128:159]}"(wq0), "=&{v[160:191]}"(wq1), "=&{v[192:223]}"(wq2), "=&{v[224:255]}"(wq3)
                 : [lo] "{v1}"(lo), [lo2] "{v2}"(lo2), [pcm1] "v"(pcm1), [hwb] "{v3}"(hwb), [vw] "{v10}"(vwp), "{v[32:35]}"(aone),
                   [cs] "s"(cs), [srdx] "s"(srdx), [srdw] "s"(srdw),
                   [nlast] "s"(5 * w + 4), [ent] "s"(ent), [foff] "v"(foff), [srdf] "s"(make_srd(ws, WS_END)), [tag] "s"(tag)
                 : "memory", "scc", "vcc", "s40", "s42", "s43", "s44", "s45", "s46", "s47", "s48", "s49", "v36", "v37", "v38", "v39",
                   CV10(4), "v56", "v57", "v58", "v59", "v64", "v65", "v66", "v67", "v68", "v69", CV10(7), CV10(8), CV10(9),
                   CV10(10), CV10(11), "v120", "v121", "v122", "v123", "v124", "v125", "v126", "v127");
  } else {
    fl = __builtin_amdgcn_raw_buffer_load_b32(wr, foff, 0, 16);
    asm volatile("" : "={v[128:159]}"(wq0), "={v[160:191]}"(wq1), "={v[192:223]}"(wq2), "={v[224:255]}"(wq3), "+{v14}"(fl));
  }
  if (__builtin_expect(!wearly, 0)) {
    while (__builtin_expect(__builtin_amdgcn_ballot_w64(fl != tag) != 0, 0)) {
      if (clock100() - tstart > T_GIVEUP) {
        for (int i = 0; i < 64; ++i) {
          const int f = (i >> 5) * 256 + w * 32 + (i & 31);
          __builtin_amdgcn_raw_buffer_store_b128(make_fragment(w1, w2, f, lane), wr, WS_WT + f * 1024 + vwp, 0, 16);
        }
        asm volatile("s_waitcnt vmcnt(0)" ::: "memory");
        break;
      }
      fl = __builtin_amdgcn_raw_buffer_load_b32(wr, foff, 0, 16);
    }
  }
  {
    unsigned tmp;
    asm volatile(R"ASM(
.macro WL r
  buffer_load_dwordx4 v[\r:\r+3], %[vw], %[srdw], %[soffw] offen sc1
  s_addk_i32 %[soffw], 0x400
.endm
.macro BIASI r, d
  v_lshlrev_b32 v[\d], 16, v[\r]
  v_and_b32 v[\d+1], s40, v[\r]
  v_lshlrev_b32 v[\d+2], 16, v[\r+1]
  v_and_b32 v[\d+3], s40, v[\r+1]
  v_lshlrev_b32 v[\d+4], 16, v[\r+2]
  v_and_b32 v[\d+5], s40, v[\r+2]
  v_lshlrev_b32 v[\d+6], 16, v[\r+3]
  v_and_b32 v[\d+7], s40, v[\r+3]
.endm
; B fragment of k-step k through the address registers v28..v31
.macro RD1 h, k, off
  ds_read_b128 v[\h:\h+3], v[28+(\k&3)] offset:(\k>>2)*256+\off
.endm
.macro ADR4 base
  v_mov_b32 v28, \base
  v_xor_b32 v29, 64, \base
  v_xor_b32 v30, 0x80, \base
  v_xor_b32 v31, 0xc0, \base
.endm
.macro MM d, a, b, c
  v_mfma_f32_16x16x32_bf16 v[\d:\d+3], v[\a:\a+3], v[\b:\b+3], v[\c:\c+3]
.endm
.macro PACK8 a
  v_cvt_pk_bf16_f32 v[\a], v[\a], v[\a+1]
  v_cvt_pk_bf16_f32 v[\a+1], v[\a+2], v[\a+3]
  v_cvt_pk_bf16_f32 v[\a+2], v[\a+4], v[\a+5]
  v_cvt_pk_bf16_f32 v[\a+3], v[\a+6], v[\a+7]
.endm
; piece n (0..5) of the GEMM1 epilogue of the eight sums v[a:a+7]: relu (NaN-propagating maximum with 0), pack, store
.macro EPI a, n, off
  .if \n < 4
  v_maximum3_f32 v[\a+2*\n], v[\a+2*\n], 0, 0
  v_maximum3_f32 v[\a+2*\n+1], v[\a+2*\n+1], 0, 0
  .elseif \n == 4
  v_cvt_pk_bf16_f32 v[\a], v[\a], v[\a+1]
  v_cvt_pk_bf16_f32 v[\a+1], v[\a+2], v[\a+3]
  .else
  v_cvt_pk_bf16_f32 v[\a+2], v[\a+4], v[\a+5]
  v_cvt_pk_bf16_f32 v[\a+3], v[\a+6], v[\a+7]
  ds_write_b128 %[tmp], v[\a:\a+3] offset:\off
  .endif
.endm

  s_mov_b32 s40, 0xffff0000
  s_cmp_lg_u32 %[wearly], 0
  s_cbranch_scc1 .Lgin_w%=
  WL 128
  WL 132
  WL 136
  WL 140
  WL 144
  WL 148
  WL 152
  WL 156
  WL 160
  WL 164
  WL 168
  WL 172
  WL 176
  WL 180
  WL 184
  WL 188
.Lgin_w%=:
  WL 192
  WL 196
  WL 200
  WL 204
  v_add_u32 %[tmp], %[b2d], %[vb1]
  ds_read_b128 v[16:19], %[vb1]
  ds_read_b128 v[20:23], %[vb1] offset:64
  ds_read_b128 v[24:27], %[tmp]
  ADR4 %[ha0]
  v_mov_b32 %[tmp], %[h1w]
  s_movk_i32 s47, 0x2000
  s_waitcnt lgkmcnt(0)
  BIASI 16, 72
  s_barrier
  ; GEMM1, GEMM2
  RD1 48, 0, 0
  RD1 52, 1, 0
  RD1 56, 2, 0
  RD1 60, 3, 0
  RD1 64, 4, 0
  RD1 68, 5, 0
  s_waitcnt vmcnt(18) lgkmcnt(5)
  MM 32, 128, 48, 72
  MM 36, 132, 48, 76
  RD1 48, 6, 0
  s_waitcnt vmcnt(16) lgkmcnt(5)
  MM 32, 136, 52, 32
  MM 36, 140, 52, 36
  RD1 52, 7, 0
  WL 208
  s_waitcnt vmcnt(15) lgkmcnt(5)
  MM 32, 144, 56, 32
  MM 36, 148, 56, 36
  RD1 56, 0, 8192
  s_waitcnt vmcnt(13) lgkmcnt(5)
  MM 32, 152, 60, 32
  MM 36, 156, 60, 36
  RD1 60, 1, 8192
  WL 212
  s_waitcnt vmcnt(12) lgkmcnt(5)
  MM 32, 160, 64, 32
  MM 36, 164, 64, 36
  RD1 64, 2, 8192
  WL 216
  s_waitcnt vmcnt(11) lgkmcnt(5)
  MM 32, 168, 68, 32
  MM 36, 172, 68, 36
  RD1 68, 3, 8192
  s_waitcnt vmcnt(9) lgkmcnt(5)
  MM 32, 176, 48, 32
  MM 36, 180, 48, 36
  RD1 48, 4, 8192
  WL 220
  s_waitcnt vmcnt(8) lgkmcnt(5)
  MM 32, 184, 52, 32
  MM 36, 188, 52, 36
  RD1 52, 5, 8192
  WL 224
  s_waitcnt lgkmcnt(5)
  MM 40, 128, 56, 72
  MM 44, 132, 56, 76
  RD1 56, 6, 8192
  s_waitcnt lgkmcnt(5)
  MM 40, 136, 60, 40
  MM 44, 140, 60, 44
  RD1 60, 7, 8192
  WL 228
  s_waitcnt lgkmcnt(5)
  MM 40, 144, 64, 40
  MM 44, 148, 64, 44
  RD1 64, 0, 16384
  EPI 32, 0, 0
  s_waitcnt lgkmcnt(5)
  MM 40, 152, 68, 40
  MM 44, 156, 68, 44
  RD1 68, 1, 16384
  EPI 32, 1, 0
  WL 232
  s_waitcnt lgkmcnt(5)
  MM 40, 160, 48, 40
  MM 44, 164, 48, 44
  RD1 48, 2, 16384
  EPI 32, 2, 0
  WL 236
  s_waitcnt lgkmcnt(5)
  MM 40, 168, 52, 40
  MM 44, 172, 52, 44
  RD1 52, 3, 16384
  EPI 32, 3, 0
  s_waitcnt lgkmcnt(5)
  MM 40, 176, 56, 40
  MM 44, 180, 56, 44
  RD1 56, 4, 16384
  EPI 32, 4, 0
  WL 240
  s_waitcnt lgkmcnt(5)
  MM 40, 184, 60, 40
  MM 44, 188, 60, 44
  RD1 60, 5, 16384
  EPI 32, 5, 0
  WL 244
  s_waitcnt lgkmcnt(6)
  MM 32, 128, 64, 72
  MM 36, 132, 64, 76
  RD1 64, 6, 16384
  s_waitcnt lgkmcnt(6)
  MM 32, 136, 68, 32
  MM 36, 140, 68, 36
  RD1 68, 7, 16384
  BIASI 20, 72
  WL 248
  s_waitcnt lgkmcnt(6)
  MM 32, 144, 48, 32
  MM 36, 148, 48, 36
  RD1 48, 0, 0
  EPI 40, 0, 16384
  s_waitcnt lgkmcnt(6)
  MM 32, 152, 52, 32
  MM 36, 156, 52, 36
  RD1 52, 1, 0
  EPI 40, 1, 16384
  WL 252
  s_waitcnt lgkmcnt(6)
  MM 32, 160, 56, 32
  MM 36, 164, 56, 36
  RD1 56, 2, 0
  EPI 40, 2, 16384
  s_add_u32 %[soffw], %[soffw], 0x40000-0x8000
  WL 80
  s_waitcnt lgkmcnt(6)
  MM 32, 168, 60, 32
  MM 36, 172, 60, 36
  RD1 60, 3, 0
  EPI 40, 3, 16384
  s_waitcnt lgkmcnt(5)
  MM 32, 176, 64, 32
  MM 36, 180, 64, 36
  RD1 64, 4, 0
  EPI 40, 4, 16384
  WL 84
  s_waitcnt lgkmcnt(5)
  MM 32, 184, 68, 32
  MM 36, 188, 68, 36
  RD1 68, 5, 0
  EPI 40, 5, 16384
  WL 88
  s_waitcnt vmcnt(17) lgkmcnt(6)
  MM 40, 192, 48, 72
  MM 44, 196, 48, 76
  RD1 48, 6, 0
  s_waitcnt vmcnt(15) lgkmcnt(6)
  MM 40, 200, 52, 40
  MM 44, 204, 52, 44
  RD1 52, 7, 0
  WL 92
  s_waitcnt vmcnt(14) lgkmcnt(6)
  MM 40, 208, 56, 40
  MM 44, 212, 56, 44
  RD1 56, 0, 8192
  EPI 32, 0, 32768
  s_waitcnt vmcnt(12) lgkmcnt(6)
  MM 40, 216, 60, 40
  MM 44, 220, 60, 44
  RD1 60, 1, 8192
  EPI 32, 1, 32768
  WL 96
  s_waitcnt vmcnt(11) lgkmcnt(6)
  MM 40, 224, 64, 40
  MM 44, 228, 64, 44
  RD1 64, 2, 8192
  EPI 32, 2, 32768
  WL 100
  s_waitcnt vmcnt(10) lgkmcnt(6)
  MM 40, 232, 68, 40
  MM 44, 236, 68, 44
  RD1 68, 3, 8192
  EPI 32, 3, 32768
  s_waitcnt vmcnt(8) lgkmcnt(5)
  MM 40, 240, 48, 40
  MM 44, 244, 48, 44
  RD1 48, 4, 8192
  EPI 32, 4, 32768
  WL 104
  s_waitcnt vmcnt(7) lgkmcnt(5)
  MM 40, 248, 52, 40
  MM 44, 252, 52, 44
  RD1 52, 5, 8192
  EPI 32, 5, 32768
  v_xor_b32 %[tmp], 64, %[tmp]
  WL 108
  s_waitcnt lgkmcnt(6)
  MM 32, 192, 56, 72
  MM 36, 196, 56, 76
  RD1 56, 6, 8192
  s_waitcnt lgkmcnt(6)
  MM 32, 200, 60, 32
  MM 36, 204, 60, 36
  RD1 60, 7, 8192
  WL 112
  s_waitcnt lgkmcnt(6)
  MM 32, 208, 64, 32
  MM 36, 212, 64, 36
  RD1 64, 0, 16384
  EPI 40, 0, 0
  s_waitcnt lgkmcnt(6)
  MM 32, 216, 68, 32
  MM 36, 220, 68, 36
  RD1 68, 1, 16384
  EPI 40, 1, 0
  WL 116
  s_waitcnt lgkmcnt(6)
  MM 32, 224, 48, 32
  MM 36, 228, 48, 36
  RD1 48, 2, 16384
  EPI 40, 2, 0
  WL 120
  s_waitcnt lgkmcnt(6)
  MM 32, 232, 52, 32
  MM 36, 236, 52, 36
  RD1 52, 3, 16384
  EPI 40, 3, 0
  s_waitcnt lgkmcnt(5)
  MM 32, 240, 56, 32
  MM 36, 244, 56, 36
  RD1 56, 4, 16384
  EPI 40, 4, 0
  WL 124
  s_waitcnt lgkmcnt(5)
  MM 32, 248, 60, 32
  MM 36, 252, 60, 36
  RD1 60, 5, 16384
  EPI 40, 5, 0
  WL 128
  s_waitcnt lgkmcnt(6)
  MM 40, 192, 64, 72
  MM 44, 196, 64, 76
  RD1 64, 6, 16384
  s_waitcnt lgkmcnt(6)
  MM 40, 200, 68, 40
  MM 44, 204, 68, 44
  RD1 68, 7, 16384
  BIASI 24, 72
  WL 132
  s_waitcnt lgkmcnt(6)
  MM 40, 208, 48, 40
  MM 44, 212, 48, 44
  EPI 32, 0, 16384
  s_waitcnt lgkmcnt(5)
  MM 40, 216, 52, 40
  MM 44, 220, 52, 44
  EPI 32, 1, 16384
  ADR4 %[hr0]
  WL 136
  s_waitcnt lgkmcnt(4)
  MM 40, 224, 56, 40
  MM 44, 228, 56, 44
  EPI 32, 2, 16384
  WL 140
  s_waitcnt lgkmcnt(3)
  MM 40, 232, 60, 40
  MM 44, 236, 60, 44
  EPI 32, 3, 16384
  s_waitcnt lgkmcnt(1)
  MM 40, 240, 64, 40
  MM 44, 244, 64, 44
  EPI 32, 4, 16384
  WL 144
  s_waitcnt lgkmcnt(0)
  MM 40, 248, 68, 40
  MM 44, 252, 68, 44
  EPI 32, 5, 16384
  s_nop 4
  EPI 40, 0, 32768
  EPI 40, 1, 32768
  EPI 40, 2, 32768
  EPI 40, 3, 32768
  EPI 40, 4, 32768
  EPI 40, 5, 32768
  WL 148
  s_waitcnt lgkmcnt(0)
  s_barrier
  RD1 208, 0, 0
  RD1 212, 0, 16384
  RD1 216, 0, 32768
  RD1 220, 1, 0
  RD1 224, 1, 16384
  RD1 228, 1, 32768
  RD1 232, 2, 0
  RD1 236, 2, 16384
  RD1 240, 2, 32768
  s_waitcnt vmcnt(16) lgkmcnt(8)
  MM 32, 80, 208, 72
  MM 36, 84, 208, 76
  s_waitcnt lgkmcnt(7)
  MM 40, 80, 212, 72
  MM 44, 84, 212, 76
  s_waitcnt lgkmcnt(6)
  MM 48, 80, 216, 72
  MM 52, 84, 216, 76
  WL 152
  WL 156
  RD1 208, 3, 0
  RD1 212, 3, 16384
  RD1 216, 3, 32768
  s_waitcnt vmcnt(16) lgkmcnt(8)
  MM 32, 88, 220, 32
  MM 36, 92, 220, 36
  s_waitcnt lgkmcnt(7)
  MM 40, 88, 224, 40
  MM 44, 92, 224, 44
  s_waitcnt lgkmcnt(6)
  MM 48, 88, 228, 48
  MM 52, 92, 228, 52
  WL 160
  WL 164
  RD1 220, 4, 0
  RD1 224, 4, 16384
  RD1 228, 4, 32768
  s_waitcnt vmcnt(16) lgkmcnt(8)
  MM 32, 96, 232, 32
  MM 36, 100, 232, 36
  s_waitcnt lgkmcnt(7)
  MM 40, 96, 236, 40
  MM 44, 100, 236, 44
  s_waitcnt lgkmcnt(6)
  MM 48, 96, 240, 48
  MM 52, 100, 240, 52
  WL 168
  WL 172
  RD1 232, 5, 0
  RD1 236, 5, 16384
  RD1 240, 5, 32768
  s_waitcnt vmcnt(16) lgkmcnt(8)
  MM 32, 104, 208, 32
  MM 36, 108, 208, 36
  s_waitcnt lgkmcnt(7)
  MM 40, 104, 212, 40
  MM 44, 108, 212, 44
  s_waitcnt lgkmcnt(6)
  MM 48, 104, 216, 48
  MM 52, 108, 216, 52
  WL 176
  WL 180
  RD1 208, 6, 0
  RD1 212, 6, 16384
  RD1 216, 6, 32768
  s_waitcnt vmcnt(16) lgkmcnt(8)
  MM 32, 112, 220, 32
  MM 36, 116, 220, 36
  s_waitcnt lgkmcnt(7)
  MM 40, 112, 224, 40
  MM 44, 116, 224, 44
  s_waitcnt lgkmcnt(6)
  MM 48, 112, 228, 48
  MM 52, 116, 228, 52
  WL 184
  WL 188
  RD1 220, 7, 0
  RD1 224, 7, 16384
  RD1 228, 7, 32768
  s_waitcnt vmcnt(16) lgkmcnt(8)
  MM 32, 120, 232, 32
  MM 36, 124, 232, 36
  s_waitcnt lgkmcnt(7)
  MM 40, 120, 236, 40
  MM 44, 124, 236, 44
  s_waitcnt lgkmcnt(6)
  MM 48, 120, 240, 48
  MM 52, 124, 240, 52
  WL 192
  WL 196
  RD1 232, 8, 0
  RD1 236, 8, 16384
  RD1 240, 8, 32768
  s_waitcnt vmcnt(16) lgkmcnt(8)
  MM 32, 128, 208, 32
  MM 36, 132, 208, 36
  s_waitcnt lgkmcnt(7)
  MM 40, 128, 212, 40
  MM 44, 132, 212, 44
  s_waitcnt lgkmcnt(6)
  MM 48, 128, 216, 48
  MM 52, 132, 216, 52
  WL 200
  WL 204
  RD1 208, 9, 0
  RD1 212, 9, 16384
  RD1 216, 9, 32768
  s_waitcnt vmcnt(16) lgkmcnt(8)
  MM 32, 136, 220, 32
  MM 36, 140, 220, 36
  s_waitcnt lgkmcnt(7)
  MM 40, 136, 224, 40
  MM 44, 140, 224, 44
  s_waitcnt lgkmcnt(6)
  MM 48, 136, 228, 48
  MM 52, 140, 228, 52
  RD1 220, 10, 0
  RD1 224, 10, 16384
  RD1 228, 10, 32768
  s_waitcnt vmcnt(14) lgkmcnt(8)
  MM 32, 144, 232, 32
  MM 36, 148, 232, 36
  s_waitcnt lgkmcnt(7)
  MM 40, 144, 236, 40
  MM 44, 148, 236, 44
  s_waitcnt lgkmcnt(6)
  MM 48, 144, 240, 48
  MM 52, 148, 240, 52
  RD1 232, 11, 0
  RD1 236, 11, 16384
  RD1 240, 11, 32768
  s_waitcnt vmcnt(12) lgkmcnt(8)
  MM 32, 152, 208, 32
  MM 36, 156, 208, 36
  s_waitcnt lgkmcnt(7)
  MM 40, 152, 212, 40
  MM 44, 156, 212, 44
  s_waitcnt lgkmcnt(6)
  MM 48, 152, 216, 48
  MM 52, 156, 216, 52
  RD1 208, 12, 0
  RD1 212, 12, 16384
  RD1 216, 12, 32768
  s_waitcnt vmcnt(10) lgkmcnt(8)
  MM 32, 160, 220, 32
  MM 36, 164, 220, 36
  s_waitcnt lgkmcnt(7)
  MM 40, 160, 224, 40
  MM 44, 164, 224, 44
  s_waitcnt lgkmcnt(6)
  MM 48, 160, 228, 48
  MM 52, 164, 228, 52
  RD1 220, 13, 0
  RD1 224, 13, 16384
  RD1 228, 13, 32768
  s_waitcnt vmcnt(8) lgkmcnt(8)
  MM 32, 168, 232, 32
  MM 36, 172, 232, 36
  s_waitcnt lgkmcnt(7)
  MM 40, 168, 236, 40
  MM 44, 172, 236, 44
  s_waitcnt lgkmcnt(6)
  MM 48, 168, 240, 48
  MM 52, 172, 240, 52
  RD1 232, 14, 0
  RD1 236, 14, 16384
  RD1 240, 14, 32768
  s_waitcnt vmcnt(6) lgkmcnt(8)
  MM 32, 176, 208, 32
  MM 36, 180, 208, 36
  s_waitcnt lgkmcnt(7)
  MM 40, 176, 212, 40
  MM 44, 180, 212, 44
  s_waitcnt lgkmcnt(6)
  MM 48, 176, 216, 48
  MM 52, 180, 216, 52
  RD1 208, 15, 0
  RD1 212, 15, 16384
  RD1 216, 15, 32768
  s_waitcnt vmcnt(4) lgkmcnt(8)
  MM 32, 184, 220, 32
  MM 36, 188, 220, 36
  s_waitcnt lgkmcnt(7)
  MM 40, 184, 224, 40
  MM 44, 188, 224, 44
  s_waitcnt lgkmcnt(6)
  MM 48, 184, 228, 48
  MM 52, 188, 228, 52
  s_waitcnt vmcnt(2) lgkmcnt(5)
  MM 32, 192, 232, 32
  MM 36, 196, 232, 36
  s_waitcnt lgkmcnt(4)
  MM 40, 192, 236, 40
  MM 44, 196, 236, 44
  s_waitcnt lgkmcnt(3)
  MM 48, 192, 240, 48
  MM 52, 196, 240, 52
  s_waitcnt vmcnt(0) lgkmcnt(2)
  MM 32, 200, 208, 32
  MM 36, 204, 208, 36
  s_waitcnt lgkmcnt(1)
  MM 40, 200, 212, 40
  MM 44, 204, 212, 44
  s_waitcnt lgkmcnt(0)
  MM 48, 200, 216, 48
  MM 52, 204, 216, 52
  s_nop 3
  PACK8 32
  buffer_store_dwordx4 v[32:35], %[ob], %[srdo], 0 offen sc1
  PACK8 40
  buffer_store_dwordx4 v[40:43], %[ob], %[srdo], s47 offen sc1
  PACK8 48
  buffer_store_dwordx4 v[48:51], %[ob2], %[srdo], 0 offen sc1
  s_nop 1
.purgem WL
.purgem BIASI
.purgem RD1
.purgem ADR4
.purgem MM
.purgem PACK8
.purgem EPI
)ASM"
                 : [tmp] "=&v"(tmp), [soffw] "+s"(soffw), "+{v[128:159]}"(wq0), "+{v[160:191]}"(wq1), "+{v[192:223]}"(wq2),
                   "+{v[224:255]}"(wq3)
                 : [vw] "{v10}"(vwp), [ha0] "{v4}"(ha0), [hr0] "{v5}"(hr0), [h1w] "{v6}"(h1w), [vb1] "{v7}"(vb1), [ob] "{v8}"(ob),
                   [ob2] "{v9}"(ob2), [srdw] "s"(srdw), [srdo] "s"(srdo), [b2d] "s"(1024 - 64 * w), [wearly] "s"(wearly), "{v14}"(fl)
                 : "memory", "scc", "s40", "s47", "v16", "v17", "v18", "v19", CV10(2), CV10(3), CV10(4), CV10(5), CV10(6), CV10(7),
                   CV10(8), CV10(9), CV10(10), CV10(11), "v120", "v121", "v122", "v123", "v124", "v125", "v126", "v127");
    if (w == 0 && vwp == 0) __hip_atomic_fetch_add((GAS unsigned*)(ws + WS_EPOCH + (blockIdx.x & 7) * 256), 1u, RLX_AGENT);
  }
}

extern "C" void kernel_launch(void* const* d_in, const int* in_sizes, int n_in, void* d_out, int out_size, void* d_ws,
                              size_t ws_size, hipStream_t stream) {
  const uint16_t* x = (const uint16_t*)d_in[0];
  const int* src = (const int*)d_in[1];
  const int* dst = (const int*)d_in[2];
  const uint16_t* eps = (const uint16_t*)d_in[3];
  const uint16_t* w1 = (const uint16_t*)d_in[4];
  const uint16_t* b1 = (const uint16_t*)d_in[5];
  const uint16_t* w2 = (const uint16_t*)d_in[6];
  const uint16_t* b2 = (const uint16_t*)d_in[7];
  unsigned char* ws = (unsigned char*)d_ws;
  (void)hipFuncSetAttribute((const void*)gin_main, hipFuncAttributeMaxDynamicSharedMemorySize, L_END);
  gin_main<<<NT, 512, L_END, stream>>>(ws, src, dst, w1, w2, x, b1, b2, eps, (uint16_t*)d_out);
}
```
